```python
import torch

def _softmax(x, axis=-1):
    return torch.softmax(x, dim=axis)

def _gelu(x):
    return torch.nn.functional.gelu(x, approximate='tanh')

def _silu(x):
    return torch.nn.functional.silu(x)

def _sigmoid(x):
    return torch.sigmoid(x)

def _layernorm(x, axis=-1, eps=1e-05):
    m = x.mean(dim=axis, keepdim=True)
    v = x.var(dim=axis, keepdim=True, unbiased=False)
    return (x - m) / torch.sqrt(v + eps)

def _scatter_add(tgt, idx, src, axis=0):
    return tgt.clone().index_add_(axis, idx, src)

def _segment_sum(src, seg, n):
    out = torch.zeros((n,) + src.shape[1:], dtype=src.dtype)
    return out.index_add_(0, seg, src)

def _pad(x, pw):
    flat = tuple((v for pair in reversed(pw) for v in pair))
    return torch.nn.functional.pad(x, flat)
kernel_name = 'adaLN_cond__gqa_block_cached__dense_activation_squared_relu__decode'

def setup_inputs():

    _w = lambda a: (lambda t: t.to(torch.bfloat16) if t.is_floating_point() else t)(torch.as_tensor(a))

    def _A_setup():
        B, Sq, D = (2, 1, 576)
        Hq, Hkv, Sk, Dh = (32, 16, 8192, 128)
        assert Hq % Hkv == 0, (Hq, Hkv)
        return {'x': _w((lambda *s: torch.randn(*s, dtype=torch.bfloat16) * 0.05)(B, Sq, D)), 'k_cache': _w((lambda *s: torch.randn(*s, dtype=torch.bfloat16) * 3.2)(B, Hkv, Sk, Dh)), 'v_cache': _w((lambda *s: torch.randn(*s, dtype=torch.bfloat16) * 0.4)(B, Hkv, Sk, Dh)), 'wq': _w((lambda *s: torch.randn(*s, dtype=torch.bfloat16) * 3.2)(D, Hq * Dh) * D ** (-0.5)), 'wo': _w((lambda *s: torch.randn(*s, dtype=torch.bfloat16) * 0.4)(Hq * Dh, D) * (Hq * Dh) ** (-0.5))}
    _A_in = _A_setup()

    def _B_setup():
        B, S, D, F = (2, 1, 576, 4608)
        return {'x': _w((lambda *s: torch.randn(*s, dtype=torch.bfloat16) * 0.05)(B, S, D)), 'w1': _w((lambda *s: torch.randn(*s, dtype=torch.bfloat16) * 0.05)(D, F)), 'b1': _w((lambda *s: torch.randn(*s, dtype=torch.bfloat16) * 0.4)(F)), 'w2': _w((lambda *s: torch.randn(*s, dtype=torch.bfloat16) * 0.05)(F, D)), 'b2': _w((lambda *s: torch.randn(*s, dtype=torch.bfloat16) * 3.2)(D))}
    _B_in = _B_setup()
    return {'A_x': _A_in['x'], 'A_k_cache': _A_in['k_cache'], 'A_v_cache': _A_in['v_cache'], 'A_wq': _A_in['wq'], 'A_wo': _A_in['wo'], 'B_x': _B_in['x'], 'B_w1': _B_in['w1'], 'B_b1': _B_in['b1'], 'B_w2': _B_in['w2'], 'B_b2': _B_in['b2'], 'c': _w((lambda *s: torch.randn(*s, dtype=torch.bfloat16) * 0.1)(2, 576) * 8.0), 'w_mod': _w((lambda *s: torch.randn(*s, dtype=torch.bfloat16) * 0.1)(576, 6 * 576) * 576 ** (-0.5) * 8.0)}

def reference(A_x, A_k_cache, A_v_cache, A_wq, A_wo, B_x, B_w1, B_b1, B_w2, B_b2, c, w_mod):

    def _A_ref(x, k_cache, v_cache, wq, wo):
        B, Sq, D = x.shape
        Hq, Dh = (32, 128)
        Hkv = k_cache.shape[1]
        rep = Hq // Hkv
        q = (lambda x, *d: x.permute(*d))((x @ wq).reshape(B, Sq, Hq, Dh), 0, 2, 1, 3)
        k = (lambda x, n, axis: torch.repeat_interleave(x, n, dim=axis))(k_cache, rep, axis=1)
        v = (lambda x, n, axis: torch.repeat_interleave(x, n, dim=axis))(v_cache, rep, axis=1)
        scores = torch.einsum('bhqd,bhkd->bhqk', q, k) * Dh ** (-0.5)
        p = _softmax(scores, axis=-1)
        o = torch.einsum('bhqk,bhkd->bhqd', p, v)
        return (lambda x, *d: x.permute(*d))(o, 0, 2, 1, 3).reshape(B, Sq, Hq * Dh) @ wo

    def _B_ref(x, w1, b1, w2, b2):
        h = torch.einsum('bsd,df->bsf', x, w1) + b1
        zero = torch.zeros_like(h)
        r = torch.where(h > zero, h, zero)
        h = r * r
        out = torch.einsum('bsf,fd->bsd', h, w2) + b2
        return out
    x = A_x
    mod = (_silu(c) @ w_mod).reshape(c.shape[0], 6, 1, -1)
    sa, ca, ga, sb, cb, gb = (mod[:, 0].clone(), mod[:, 1].clone(), mod[:, 2].clone(), mod[:, 3].clone(), mod[:, 4].clone(), mod[:, 5].clone())
    h = _layernorm(x, axis=-1) * (1 + ca) + sa
    x = x + ga * _A_ref(h, A_k_cache, A_v_cache, A_wq, A_wo)
    h = _layernorm(x, axis=-1) * (1 + cb) + sb
    x = x + gb * _B_ref(h, B_w1, B_b1, B_w2, B_b2)
    return x
```

```cpp
#include <hip/hip_runtime.h>
#include <hip/hip_bf16.h>
#include <stdint.h>

typedef unsigned short u16;
typedef unsigned u32x4 __attribute__((ext_vector_type(4)));
typedef __bf16 bf16x2 __attribute__((ext_vector_type(2)));

constexpr int B = 2, D = 576, HQ = 32, HKV = 16, SK = 8192, DH = 128, F = 4608;
constexpr int NSPLIT = 8, TOK = SK / NSPLIT;

__device__ __forceinline__ float bf2f(u16 v) { return __builtin_bit_cast(float, (unsigned)v << 16); }
__device__ __forceinline__ float rbf(float f) { return (float)(__bf16)f; }
__device__ __forceinline__ u16 f2bf(float f) { return __builtin_bit_cast(u16, (__bf16)f); }
__device__ __forceinline__ float lo16(unsigned v) { return __builtin_bit_cast(float, v << 16); }
__device__ __forceinline__ float hi16(unsigned v) { return __builtin_bit_cast(float, v & 0xffff0000u); }
__device__ __forceinline__ float dot2(unsigned k, unsigned q, float acc) {
  return __builtin_amdgcn_fdot2_f32_bf16(__builtin_bit_cast(bf16x2, k), __builtin_bit_cast(bf16x2, q), acc, false);
}

__device__ void gemv16(const float* sin0, const float* sin1, const u16* W, int K, int N, int col0, float* red, float out[2]) {
  int t = threadIdx.x, half = t & 1, r0 = t >> 1;
  float a0[8], a1[8];
  for (int j = 0; j < 8; ++j) { a0[j] = 0.f; a1[j] = 0.f; }
  for (int r = r0; r < K; r += 128) {
    u32x4 w = *(const u32x4*)(W + (size_t)r * N + col0 + half * 8);
    float x0 = sin0[r], x1 = sin1[r];
    for (int j = 0; j < 4; ++j) {
      float wl = lo16(w[j]), wh = hi16(w[j]);
      a0[2 * j] += x0 * wl; a0[2 * j + 1] += x0 * wh;
      a1[2 * j] += x1 * wl; a1[2 * j + 1] += x1 * wh;
    }
  }
  for (int j = 0; j < 8; ++j) { red[r0 * 33 + half * 8 + j] = a0[j]; red[r0 * 33 + 16 + half * 8 + j] = a1[j]; }
  __syncthreads();
  float s = 0.f;
  if (t < 32) { for (int r = 0; r < 128; ++r) s += red[r * 33 + t]; }
  out[0] = s;
  __syncthreads();
}

__global__ void __launch_bounds__(256) k_mod(const u16* c, const u16* wmod, float* mod) {
  __shared__ float s0[D], s1[D], red[128 * 33];
  for (int i = threadIdx.x; i < D; i += 256) {
    float v0 = bf2f(c[i]), v1 = bf2f(c[D + i]);
    s0[i] = rbf(v0 / (1.f + __expf(-v0))); s1[i] = rbf(v1 / (1.f + __expf(-v1)));
  }
  __syncthreads();
  float o[2];
  gemv16(s0, s1, wmod, D, 6 * D, blockIdx.x * 16, red, o);
  if (threadIdx.x < 32) mod[(threadIdx.x >> 4) * 6 * D + blockIdx.x * 16 + (threadIdx.x & 15)] = rbf(o[0]);
}

__device__ void adaln(float* xs, const float* sh, const float* sc, float* tmp) {
  int t = threadIdx.x, w = t >> 6, lane = t & 63;
  if (w < 2) {
    float* x = xs + w * D;
    float s = 0.f;
    for (int i = lane; i < D; i += 64) s += x[i];
    for (int o = 32; o; o >>= 1) s += __shfl_xor(s, o);
    float mean = s / D;
    float v = 0.f;
    for (int i = lane; i < D; i += 64) { float d = x[i] - mean; v += d * d; }
    for (int o = 32; o; o >>= 1) v += __shfl_xor(v, o);
    float m = rbf(mean), var = rbf(v / D);
    float den = rbf(sqrtf(rbf(var + 1e-5f)));
    for (int i = lane; i < D; i += 64) {
      float ln = rbf(rbf(x[i] - m) / den);
      float h = rbf(rbf(ln * rbf(1.f + sc[w * 6 * D + i])) + sh[w * 6 * D + i]);
      tmp[w * D + i] = h;
    }
  }
  __syncthreads();
}

__global__ void __launch_bounds__(256) k_q(const u16* x, const float* mod, const u16* wq, u16* q) {
  __shared__ float xs[2 * D], hs[2 * D], red[128 * 33];
  for (int i = threadIdx.x; i < 2 * D; i += 256) xs[i] = bf2f(x[i]);
  __syncthreads();
  adaln(xs, mod, mod + D, hs);
  float o[2];
  gemv16(hs, hs + D, wq, D, HQ * DH, blockIdx.x * 16, red, o);
  if (threadIdx.x < 32) q[(threadIdx.x >> 4) * HQ * DH + blockIdx.x * 16 + (threadIdx.x & 15)] = f2bf(o[0]);
}

template <int CTRL> __device__ __forceinline__ float dpp(float x) {
  return __builtin_bit_cast(float, __builtin_amdgcn_mov_dpp(__builtin_bit_cast(int, x), CTRL, 0xf, 0xf, true));
}
constexpr int XOR1 = 0xB1, XOR2 = 0x4E, XOR7 = 0x141, XOR8 = 0x128;

__global__ void __launch_bounds__(512) k_attn(const u16* q, const u16* kc, const u16* vc, float* part) {
  __shared__ float sc[2][TOK];
  __shared__ float red[8][2][DH];
  __shared__ float mx[8][2], sm[8][2];
  int blk = blockIdx.x, s = blk % NSPLIT, kvh = (blk / NSPLIT) % HKV, b = blk / (NSPLIT * HKV);
  int t = threadIdx.x, w = t >> 6, lane = t & 63, r = lane >> 4, c = lane & 15;
  u32x4 q0 = *(const u32x4*)(q + b * HQ * DH + (2 * kvh) * DH + 8 * c);
  u32x4 q1 = *(const u32x4*)(q + b * HQ * DH + (2 * kvh + 1) * DH + 8 * c);
  size_t base = ((size_t)(b * HKV + kvh) * SK + s * TOK + w * 128) * DH;
  const u32x4* kp = (const u32x4*)(kc + base) + lane;
  const u32x4* vp = (const u32x4*)(vc + base) + lane;
  float m0 = -INFINITY, m1 = -INFINITY;
#pragma unroll 8
  for (int i = 0; i < 32; ++i) {
    u32x4 k = __builtin_nontemporal_load(kp + i * 64);
    float d0 = 0.f, d1 = 0.f;
    for (int j = 0; j < 4; ++j) { d0 = dot2(k[j], q0[j], d0); d1 = dot2(k[j], q1[j], d1); }
    d0 += dpp<XOR8>(d0); d1 += dpp<XOR8>(d1);
    d0 += dpp<XOR7>(d0); d1 += dpp<XOR7>(d1);
    d0 += dpp<XOR2>(d0); d1 += dpp<XOR2>(d1);
    d0 += dpp<XOR1>(d0); d1 += dpp<XOR1>(d1);
    d0 = rbf(rbf(d0) * 0.08838834764831845f); d1 = rbf(rbf(d1) * 0.08838834764831845f);
    m0 = fmaxf(m0, d0); m1 = fmaxf(m1, d1);
    if (c == 0) { sc[0][w * 128 + i * 4 + r] = d0; sc[1][w * 128 + i * 4 + r] = d1; }
  }
  for (int o = 32; o >= 16; o >>= 1) { m0 = fmaxf(m0, __shfl_xor(m0, o)); m1 = fmaxf(m1, __shfl_xor(m1, o)); }
  float l0 = 0.f, l1 = 0.f;
  for (int i = lane; i < 128; i += 64) {
    float p0 = __expf(sc[0][w * 128 + i] - m0), p1 = __expf(sc[1][w * 128 + i] - m1);
    sc[0][w * 128 + i] = p0; sc[1][w * 128 + i] = p1; l0 += p0; l1 += p1;
  }
  for (int o = 32; o; o >>= 1) { l0 += __shfl_xor(l0, o); l1 += __shfl_xor(l1, o); }
  float a0[8], a1[8];
  for (int j = 0; j < 8; ++j) { a0[j] = 0.f; a1[j] = 0.f; }
#pragma unroll 8
  for (int i = 0; i < 32; ++i) {
    u32x4 v = __builtin_nontemporal_load(vp + i * 64);
    float p0 = sc[0][w * 128 + i * 4 + r], p1 = sc[1][w * 128 + i * 4 + r];
    for (int j = 0; j < 4; ++j) {
      float vl = lo16(v[j]), vh = hi16(v[j]);
      a0[2 * j] += p0 * vl; a0[2 * j + 1] += p0 * vh;
      a1[2 * j] += p1 * vl; a1[2 * j + 1] += p1 * vh;
    }
  }
  for (int j = 0; j < 8; ++j) {
    a0[j] += __shfl_xor(a0[j], 16); a0[j] += __shfl_xor(a0[j], 32);
    a1[j] += __shfl_xor(a1[j], 16); a1[j] += __shfl_xor(a1[j], 32);
  }
  if (r == 0) for (int j = 0; j < 8; ++j) { red[w][0][8 * c + j] = a0[j]; red[w][1][8 * c + j] = a1[j]; }
  if (lane == 0) { mx[w][0] = m0; mx[w][1] = m1; sm[w][0] = l0; sm[w][1] = l1; }
  __syncthreads();
  if (t < 256) {
    int h = t >> 7, d = t & 127;
    float M = -INFINITY;
    for (int i = 0; i < 8; ++i) M = fmaxf(M, mx[i][h]);
    float L = 0.f, A = 0.f;
    for (int i = 0; i < 8; ++i) { float e = __expf(mx[i][h] - M); L += e * sm[i][h]; A += e * red[i][h][d]; }
    float* pp = part + ((size_t)(b * HQ + 2 * kvh + h) * NSPLIT + s) * 132;
    pp[4 + d] = A;
    if (d == 0) { pp[0] = M; pp[1] = L; }
  }
}

__global__ void __launch_bounds__(256) k_wo(const float* part, const u16* wo, const u16* x, const float* mod, float* x1) {
  __shared__ float os[2 * HQ * DH], red[128 * 33];
  for (int i = threadIdx.x; i < 2 * HQ * DH; i += 256) {
    int bh = i >> 7, d = i & 127;
    const float* pp = part + (size_t)bh * NSPLIT * 132;
    float M = -INFINITY;
    for (int s = 0; s < NSPLIT; ++s) M = fmaxf(M, pp[s * 132]);
    float L = 0.f, A = 0.f;
    for (int s = 0; s < NSPLIT; ++s) { float e = __expf(pp[s * 132] - M); L += e * pp[s * 132 + 1]; A += e * pp[s * 132 + 4 + d]; }
    os[i] = rbf(A / L);
  }
  __syncthreads();
  float o[2];
  gemv16(os, os + HQ * DH, wo, HQ * DH, D, blockIdx.x * 16, red, o);
  if (threadIdx.x < 32) {
    int b = threadIdx.x >> 4, col = blockIdx.x * 16 + (threadIdx.x & 15);
    float a = rbf(o[0]);
    x1[b * D + col] = rbf(bf2f(x[b * D + col]) + rbf(mod[b * 6 * D + 2 * D + col] * a));
  }
}

__global__ void __launch_bounds__(256) k_w1(const float* x1, const float* mod, const u16* w1, const u16* b1, float* h1) {
  __shared__ float xs[2 * D], hs[2 * D], red[128 * 33];
  for (int i = threadIdx.x; i < 2 * D; i += 256) xs[i] = x1[i];
  __syncthreads();
  adaln(xs, mod + 3 * D, mod + 4 * D, hs);
  float o[2];
  gemv16(hs, hs + D, w1, D, F, blockIdx.x * 16, red, o);
  if (threadIdx.x < 32) {
    int b = threadIdx.x >> 4, col = blockIdx.x * 16 + (threadIdx.x & 15);
    float h = rbf(rbf(o[0]) + bf2f(b1[col]));
    float r = h > 0.f ? h : 0.f;
    h1[b * F + col] = rbf(r * r);
  }
}

__global__ void __launch_bounds__(256) k_w2(const float* h1, const float* x1, const float* mod, const u16* w2, const u16* b2, u16* out) {
  __shared__ float hs[2 * F], red[128 * 33];
  for (int i = threadIdx.x; i < 2 * F; i += 256) hs[i] = h1[i];
  __syncthreads();
  float o[2];
  gemv16(hs, hs + F, w2, F, D, blockIdx.x * 16, red, o);
  if (threadIdx.x < 32) {
    int b = threadIdx.x >> 4, col = blockIdx.x * 16 + (threadIdx.x & 15);
    float y = rbf(rbf(o[0]) + bf2f(b2[col]));
    out[b * D + col] = f2bf(x1[b * D + col] + rbf(mod[b * 6 * D + 5 * D + col] * y));
  }
}

extern "C" void kernel_launch(void* const* d_in, const int* in_sizes, int n_in, void* d_out, int out_size, void* d_ws, size_t ws_size, hipStream_t stream) {
  const u16* x = (const u16*)d_in[0];
  const u16* kc = (const u16*)d_in[1];
  const u16* vc = (const u16*)d_in[2];
  const u16* wq = (const u16*)d_in[3];
  const u16* wo = (const u16*)d_in[4];
  const u16* w1 = (const u16*)d_in[6];
  const u16* b1 = (const u16*)d_in[7];
  const u16* w2 = (const u16*)d_in[8];
  const u16* b2 = (const u16*)d_in[9];
  const u16* c = (const u16*)d_in[10];
  const u16* wmod = (const u16*)d_in[11];
  char* ws = (char*)d_ws;
  float* mod = (float*)ws;
  u16* q = (u16*)(ws + 32768);
  float* part = (float*)(ws + 65536);
  float* x1 = (float*)(ws + 65536 + 524288);
  float* h1 = (float*)(ws + 65536 + 524288 + 8192);
  k_mod<<<6 * D / 16, 256, 0, stream>>>(c, wmod, mod);
  k_q<<<HQ * DH / 16, 256, 0, stream>>>(x, mod, wq, q);
  k_attn<<<B * HKV * NSPLIT, 512, 0, stream>>>(q, kc, vc, part);
  k_wo<<<D / 16, 256, 0, stream>>>(part, wo, x, mod, x1);
  k_w1<<<F / 16, 256, 0, stream>>>(x1, mod, w1, b1, h1);
  k_w2<<<D / 16, 256, 0, stream>>>(h1, x1, mod, w2, b2, (u16*)d_out);
}
```

```cpp
#define BACKOFF 4
#define LO 3
#define FDELAY 300
#define CODEPF 28900
#define T3DELAY 20
#define KSH 4
#define WOAHEAD 10
#define T1DELAY 20
#define T2DELAY 130
#define NORG 4
#define PLAN0 280
#define QLEAD 11
#define BOOST 60
#define TEND 2070
#define C0X2 11
#define ORGT0 600
#define ORGDT 390
#define KGO 250
#define NPARK_ 52
#define HDELAY 460
#define BETA 3
#define BETA0 3
#define ADAPT 1
#define HAFTER 30
#define T1COMMON 135
#define T2COMMON 255
#include <hip/hip_runtime.h>
#include <stdint.h>

namespace {
typedef unsigned short u16;
typedef unsigned u32;
typedef unsigned long long u64;
typedef u32 u32x2 __attribute__((ext_vector_type(2)));
typedef u32 u32x4 __attribute__((ext_vector_type(4)));
typedef float f32x2 __attribute__((ext_vector_type(2)));
typedef float f32x4 __attribute__((ext_vector_type(4)));
typedef __bf16 bf16x8 __attribute__((ext_vector_type(8)));
typedef __bf16 bf16x2 __attribute__((ext_vector_type(2)));
typedef short s16x4 __attribute__((ext_vector_type(4)));
#define LAS __attribute__((address_space(3)))
typedef __amdgpu_buffer_rsrc_t rsrc_t;

#define ORGBIAS 0
#define ORG 1

#define W2PRE 12
#define ORGLAG0 230
#define ORGLAG 120
#define CRITHOLD 0
#define ENDT 6
#define ENDSLACK 0
#define TLBG 0
#define CRITVM 24
#define XA 0
constexpr int D = 576, HQ = 32, HKV = 16, SK = 8192, DH = 128, FF = 4608, NMOD = 6 * D;
constexpr int AUX_NT = 2, AUX_SC1 = 16;
constexpr u32 SENT = 0xFFFFFFFFu;
constexpr int SPIN_CAP = 1 << 18;
constexpr float LOG2E = 1.4426950408889634f;

constexpr int W_EPOCH = 0;
constexpr int W_ORG = 1 << 21;
constexpr int W_RDY = 4096;
constexpr int W_MODF = 8192;
constexpr int W_ML = 32768;
constexpr int W_MP = 65536;
constexpr int W_QF = W_MP + 3 * 216 * 128;
constexpr int W_T1 = W_QF + 32 * 3072;
constexpr int W_T2 = W_T1 + 256 * 1024;
constexpr int W_T3 = W_T2 + 8 * 36 * 128;
constexpr int T3_RED = 145 * 128;
constexpr int W_ORG_ = W_T3 + 36 * T3_RED;
constexpr int W_END = W_ORG_ + 8 * 4096;
constexpr int W_STAMP = 4 << 20;

constexpr int L_WM = 0, L_WT = 24576, L_MODF = L_WT + 73728, L_LNX = L_MODF + 9216, L_XB = L_LNX + 4608, L_P = L_XB + 2304, L_ACC = L_P + 9216,
              L_SILU = L_ACC + 4096, L_HS = L_SILU + 768, L_Q = L_HS + 2304, L_ZERO = L_Q + 512, L_ML = L_ZERO + 512,
              L_SC = L_ML + 64, L_TAG = L_SC + 32, L_MB = L_TAG + 8, L_BIAS = L_SC + 64, L_FLAG = L_BIAS + 256, L_END = L_FLAG + 256;
constexpr int XI_STR = 1312;
constexpr int L_XI = 0, L_O = L_LNX, L_RED = 0, L_H1P = L_XI + 8 * XI_STR, L_ST = L_H1P + 3456, L_X1 = L_ST + 128;
static_assert(L_END <= 163840 && L_X1 + 4608 <= L_WT, "LDS");
constexpr int F_LN = 0, F_SI = 1, F_Q = 2, F_SC = 3, F_ML = 4, F_W = 8, F_LAST = 10, F_PUB = 11, F_CNT = 12, F_CLR = 13, F_V = 14, F_SMP = 15, F_ORG = 16, F_RDY = 17, F_WQ = 20, F_H = 24, F_WD = 28, F_HG = 32, F_WS = 36, F_CI = 37, F_NEAR = 18;

struct Params {
  const void* first[8];
  const void *kc, *vc, *b1, *w2, *b2;
  void *out, *ws;
  const void* w1;
  u32 job[32][8][2];
};
constexpr int J_MOD = 0, J_WO = 1, J_WQ = 2, J_W1 = 3;

__device__ __forceinline__ float bflo(u32 v) { return __builtin_bit_cast(float, v << 16); }
__device__ __forceinline__ float bfhi(u32 v) { return __builtin_bit_cast(float, v & 0xffff0000u); }
__device__ __forceinline__ float rbf(float f) { return (float)(__bf16)f; }
__device__ __forceinline__ u32 f2bf(float f) { return (u32)__builtin_bit_cast(u16, (__bf16)f); }
__device__ __forceinline__ u32 pk2bf(float lo, float hi) { bf16x2 v = {(__bf16)lo, (__bf16)hi}; return __builtin_bit_cast(u32, v); }
__device__ __forceinline__ u32 fbits(float f) { return __builtin_bit_cast(u32, f); }
__device__ __forceinline__ float bitsf(u32 v) { return __builtin_bit_cast(float, v); }
__device__ __forceinline__ f32x4 asf(u32x4 v) { return __builtin_bit_cast(f32x4, v); }
__device__ __forceinline__ float rcp(float x) { return __builtin_amdgcn_rcpf(x); }
template <typename P> __device__ __forceinline__ rsrc_t mkrs(P p, int bytes) { return __builtin_amdgcn_make_buffer_rsrc((void*)(u64)p, 0, bytes, 0x00020000); }
__device__ __forceinline__ f32x4 mfma(u32x4 a, u32x4 b, f32x4 c) {
  return __builtin_amdgcn_mfma_f32_16x16x32_bf16(__builtin_bit_cast(bf16x8, a), __builtin_bit_cast(bf16x8, b), c, 0, 0, 0);
}
template <int CTRL> __device__ __forceinline__ float dpp(float x) {
  return __builtin_bit_cast(float, __builtin_amdgcn_mov_dpp(__builtin_bit_cast(int, x), CTRL, 0xf, 0xf, true));
}
constexpr int XOR1 = 0xB1, XOR2 = 0x4E, XOR7 = 0x141, XOR8 = 0x128, ROR4 = 0x124;
__device__ __forceinline__ float x16_sum(float x) { auto s = __builtin_amdgcn_permlane16_swap(fbits(x), fbits(x), false, false); return bitsf(s[0]) + bitsf(s[1]); }
__device__ __forceinline__ float x32_sum(float x) { auto s = __builtin_amdgcn_permlane32_swap(fbits(x), fbits(x), false, false); return bitsf(s[0]) + bitsf(s[1]); }
__device__ __forceinline__ float x32_max(float x) { auto s = __builtin_amdgcn_permlane32_swap(fbits(x), fbits(x), false, false); return fmaxf(bitsf(s[0]), bitsf(s[1])); }
__device__ __forceinline__ float row_sum(float x) { x += dpp<XOR8>(x); x += dpp<XOR7>(x); x += dpp<XOR2>(x); x += dpp<XOR1>(x); return x; }
__device__ __forceinline__ float row_max(float x) { x = fmaxf(x, dpp<XOR8>(x)); x = fmaxf(x, dpp<XOR7>(x)); x = fmaxf(x, dpp<XOR2>(x)); x = fmaxf(x, dpp<XOR1>(x)); return x; }
__device__ __forceinline__ float wave_sum(float x) { return x32_sum(x16_sum(row_sum(x))); }
__device__ __forceinline__ u32 nosent(float v) { return min(fbits(v), SENT - 1); }
__device__ __forceinline__ u32x4 nosent(f32x4 v) { return u32x4{nosent(v[0]), nosent(v[1]), nosent(v[2]), nosent(v[3])}; }
__device__ __forceinline__ bool empty(u32 v) { return v == SENT; }
__device__ __forceinline__ bool empty(u32x2 v) { return max(v[0], v[1]) == SENT; }
__device__ __forceinline__ bool empty(u32x4 v) { return max(max(v[0], v[1]), max(v[2], v[3])) == SENT; }
__device__ __forceinline__ void ldsc(u32& v, rsrc_t rs, int vo, int so) { v = __builtin_amdgcn_raw_buffer_load_b32(rs, vo, so, AUX_SC1); }
__device__ __forceinline__ void ldsc(u32x2& v, rsrc_t rs, int vo, int so) { v = __builtin_amdgcn_raw_buffer_load_b64(rs, vo, so, AUX_SC1); }
__device__ __forceinline__ void ldsc(u32x4& v, rsrc_t rs, int vo, int so) { v = __builtin_amdgcn_raw_buffer_load_b128(rs, vo, so, AUX_SC1); }
__device__ __forceinline__ void stsc(u32 v, rsrc_t rs, int vo) { __builtin_amdgcn_raw_buffer_store_b32(v, rs, vo, 0, AUX_SC1); }
__device__ __forceinline__ void stsc(u32x2 v, rsrc_t rs, int vo) { __builtin_amdgcn_raw_buffer_store_b64(v, rs, vo, 0, AUX_SC1); }
__device__ __forceinline__ void stsc(u32x4 v, rsrc_t rs, int vo) { __builtin_amdgcn_raw_buffer_store_b128(v, rs, vo, 0, AUX_SC1); }

#define VM0() asm volatile("s_waitcnt vmcnt(0)" ::: "memory")
#define LGKM0() asm volatile("s_waitcnt lgkmcnt(0)" ::: "memory")
#define BAR() do { asm volatile("s_waitcnt lgkmcnt(0)" ::: "memory"); __builtin_amdgcn_s_barrier(); asm volatile("" ::: "memory"); } while (0)
#define PIN(x) asm volatile("" : "+v"(x))

#define NOTE(i, v) do { (void)(v); } while (0)
#define STAMP(i) do {} while (0)

template <typename T> __device__ __forceinline__ LAS T* lp(LAS unsigned char* L, int off) { return (LAS T*)(L + off); }
__device__ __forceinline__ u32x4 tr_frag(LAS unsigned char* L, int addr, int rowbytes) {
  s16x4 a = __builtin_amdgcn_ds_read_tr16_b64_v4i16((LAS s16x4*)(L + addr));
  s16x4 b = __builtin_amdgcn_ds_read_tr16_b64_v4i16((LAS s16x4*)(L + addr + 4 * rowbytes));
  return __builtin_bit_cast(u32x4, __builtin_shufflevector(a, b, 0, 1, 2, 3, 4, 5, 6, 7));
}
template <int LSTEP = 1024> __device__ __forceinline__ void dma_rows(rsrc_t rs, int lds, int voff, int so, int step, int n4) {
  asm volatile(
      "s_cmp_eq_u32 %[n], 0\n\ts_cbranch_scc1 2f\n\ts_mov_b32 m0, %[lds]\n\ts_nop 4\n"
      "1:\n\t"
      "buffer_load_dwordx4 %[vo], %[rs], %[so] offen nt lds\n\ts_add_u32 m0, m0, %[ls]\n\ts_add_u32 %[so], %[so], %[st]\n\t"
      "buffer_load_dwordx4 %[vo], %[rs], %[so] offen nt lds\n\ts_add_u32 m0, m0, %[ls]\n\ts_add_u32 %[so], %[so], %[st]\n\t"
      "buffer_load_dwordx4 %[vo], %[rs], %[so] offen nt lds\n\ts_add_u32 m0, m0, %[ls]\n\ts_add_u32 %[so], %[so], %[st]\n\t"
      "buffer_load_dwordx4 %[vo], %[rs], %[so] offen nt lds\n\ts_add_u32 m0, m0, %[ls]\n\ts_add_u32 %[so], %[so], %[st]\n\t"
      "s_sub_u32 %[n], %[n], 1\n\ts_cmp_lg_u32 %[n], 0\n\ts_cbranch_scc1 1b\n2:"
      : [so] "+s"(so), [n] "+s"(n4) : [lds] "s"(lds), [vo] "v"(voff), [rs] "s"(rs), [st] "s"(step), [ls] "n"(LSTEP) : "memory", "scc");
}
__device__ __forceinline__ void gate(u32& due, u32 slope, u32 ksh) {
  u32 t;
  asm volatile(
      "s_waitcnt vmcnt(8)\n"
      "1:\n\ts_memrealtime vcc\n\ts_getreg_b32 %0, hwreg(HW_REG_IB_STS, 0, 4)\n\ts_cmp_lt_u32 %0, %2\n\ts_waitcnt lgkmcnt(0)\n\ts_cbranch_scc1 2f\n\t"
      "s_lshl_b32 %0, %0, %3\n\ts_lshl_b32 vcc_lo, vcc_lo, 6\n\ts_sub_i32 vcc_lo, vcc_lo, %1\n\ts_cmp_lt_i32 vcc_lo, %0\n\ts_cbranch_scc1 1b\n"
      "2:\n\ts_add_u32 %1, %1, %4"
      : "=&s"(t), "+s"(due) : "n"(LO), "s"(ksh), "s"(slope) : "memory", "scc", "vcc");
}
__device__ __forceinline__ u32 clock32() { u64 t; asm volatile("s_memrealtime %0\n\ts_waitcnt lgkmcnt(0)" : "=s"(t) :: "memory"); return (u32)t; }
__device__ __forceinline__ void pause_until(u32 t) { while ((int)(clock32() - t) < 0) {} }
__device__ __forceinline__ void lds_wait(LAS unsigned char* L, int f, int n = 1) {
  volatile LAS u32* fl = (volatile LAS u32*)(L + L_FLAG);
#pragma unroll 1
  for (int it = 0; it < (SPIN_CAP << 4); ++it) {
    u32 v = fl[f];
    for (int k = 1; k < n; ++k) v &= fl[f + k];
    if (__builtin_amdgcn_readfirstlane(v) != 0u) break;
    __builtin_amdgcn_s_sleep(1);
  }
  asm volatile("" ::: "memory");
}
__device__ __forceinline__ void lds_set(LAS unsigned char* L, int f) {
  LGKM0();
  ((volatile LAS u32*)(L + L_FLAG))[f] = 1u;
}
struct Tag { u32 a, b; };
__device__ __forceinline__ Tag make_tag(u32x2 e) {
  const u32 e0 = __builtin_amdgcn_readfirstlane(e[0]);
  u64 z = (((u64)(u32)__builtin_amdgcn_readfirstlane(e[1]) << 32) | e0) >> 4;
  z += 0x9E3779B97F4A7C15ull; z = (z ^ (z >> 30)) * 0xBF58476D1CE4E5B9ull; z = (z ^ (z >> 27)) * 0x94D049BB133111EBull; z ^= z >> 31;
  const u32 r = (e0 & 0xfu) * 0x11111111u;
  return Tag{(u32)z ^ r, (u32)(z >> 32) ^ r};
}
__device__ __forceinline__ u32x4 tag_bytes(Tag t, bool odd) { const u32 w = odd ? t.b : t.a; return u32x4{w & 0xffu, (w >> 8) & 0xffu, (w >> 16) & 0xffu, w >> 24}; }
__device__ __forceinline__ u32x4 put_tag(f32x4 v, u32x4 tb) {
  return u32x4{(fbits(v[0]) & ~0xffu) | tb[0], (fbits(v[1]) & ~0xffu) | tb[1], (fbits(v[2]) & ~0xffu) | tb[2], (fbits(v[3]) & ~0xffu) | tb[3]};
}
__device__ __forceinline__ u32x4 tag_nibs(Tag t, int q) { const u32 w = ((q & 2) ? t.b : t.a) >> ((q & 1) * 16); return u32x4{w & 15u, (w >> 4) & 15u, (w >> 8) & 15u, (w >> 12) & 15u}; }
__device__ __forceinline__ u32x4 put_nibs(f32x4 v, u32x4 tn) { return ((__builtin_bit_cast(u32x4, v) + 8u) & ~15u) | tn; }
template <int N, typename F, typename H>
__device__ __forceinline__ int gather_nibs(rsrc_t ws, F off, u32x4 tn, f32x4 (&out)[N], H&& meanwhile) {
  int it = 0;
#pragma unroll 1
  for (; it < SPIN_CAP; ++it) {
    asm volatile("" ::: "memory");
    u32x4 v[N];
#pragma unroll
    for (int i = 0; i < N; ++i) { int vo, so; off(i, vo, so); ldsc(v[i], ws, vo, so); }
    if (it == 0) meanwhile();
    u32 bad = 0;
#pragma unroll
    for (int i = 0; i < N; ++i) { v[i] ^= tn; bad |= (v[i][0] | v[i][1]) | (v[i][2] | v[i][3]); out[i] = asf(v[i]); }
    if (!__any((bad & 15u) != 0u)) break;
    __builtin_amdgcn_s_sleep(BACKOFF);
  }
  return it;
}
template <int N, typename F>
__device__ __forceinline__ int gather_tag(rsrc_t ws, F off, Tag tg, u32x4 (&v)[N]) {
  int it = 0;
#pragma unroll 1
  for (; it < SPIN_CAP; ++it) {
    asm volatile("" ::: "memory");
#pragma unroll
    for (int i = 0; i < N; ++i) { int vo, so; off(i, vo, so); ldsc(v[i], ws, vo, so); }
    u32 bad = 0;
#pragma unroll
    for (int i = 0; i < N; ++i) bad |= (v[i][1] ^ tg.a) | (v[i][3] ^ tg.b);
    if (!__any(bad != 0u)) break;
    __builtin_amdgcn_s_sleep(BACKOFF);
  }
  return it;
}
__device__ __forceinline__ void ldm(u32& v, u64 m, rsrc_t rs, int vo, int so) {
  asm volatile("s_mov_b64 exec, %1\n\tbuffer_load_dword %0, %2, %3, %4 offen sc1\n\ts_mov_b64 exec, -1" : "+v"(v) : "s"(m), "v"(vo), "s"(rs), "s"(so) : "memory");
}
__device__ __forceinline__ void ldm(u32x2& v, u64 m, rsrc_t rs, int vo, int so) {
  asm volatile("s_mov_b64 exec, %1\n\tbuffer_load_dwordx2 %0, %2, %3, %4 offen sc1\n\ts_mov_b64 exec, -1" : "+v"(v) : "s"(m), "v"(vo), "s"(rs), "s"(so) : "memory");
}
__device__ __forceinline__ void ldm(u32x4& v, u64 m, rsrc_t rs, int vo, int so) {
  asm volatile("s_mov_b64 exec, %1\n\tbuffer_load_dwordx4 %0, %2, %3, %4 offen sc1\n\ts_mov_b64 exec, -1" : "+v"(v) : "s"(m), "v"(vo), "s"(rs), "s"(so) : "memory");
}
__device__ __forceinline__ float tof(u32 v) { return bitsf(v); }
__device__ __forceinline__ f32x4 tof(u32x4 v) { return asf(v); }
__device__ __forceinline__ float hsum(float v) { return v; }
__device__ __forceinline__ float hsum(f32x4 v) { return (v[0] + v[2]) + (v[1] + v[3]); }
template <int A, int B, typename T, int N> __device__ __forceinline__ auto tree(const T (&v)[N]) {
  if constexpr (B - A == 1) return tof(v[A]); else return tree<A, (A + B + 1) / 2>(v) + tree<(A + B + 1) / 2, B>(v);
}
template <int N, typename T, typename H>
__device__ __forceinline__ auto gather_sum(rsrc_t ws, int voff, int stride, H&& meanwhile) {
  T v[N];
#pragma unroll
  for (int i = 0; i < N; ++i) { asm volatile("" : "=v"(v[i])); ldm(v[i], ~0ull, ws, voff, i * stride); }
  meanwhile();
  decltype(tof(v[0])) sum;
#pragma unroll 1
  for (int it = 0; it < SPIN_CAP; ++it) {
    VM0();
#pragma unroll
    for (int i = 0; i < N; ++i) PIN(v[i]);
    sum = tree<0, N>(v);
    const float t = hsum(sum);
    if (__builtin_amdgcn_ballot_w64(t != t) == 0) break;
    u64 m[N], any = 0;
#pragma unroll
    for (int i = 0; i < N; ++i) { m[i] = __builtin_amdgcn_ballot_w64(empty(v[i])); any |= m[i]; }
    if (any == 0) break;
#pragma unroll
    for (int i = 0; i < N; ++i) { ldm(v[i], m[i], ws, voff, i * stride); }
  }
  VM0();
  return sum;
}
template <int N>
__device__ __forceinline__ float gather_sum_tag(rsrc_t ws, int voff, int stride, u32 tb) {
  u32 v[N], x[N];
#pragma unroll
  for (int i = 0; i < N; ++i) { asm volatile("" : "=v"(v[i])); ldm(v[i], ~0ull, ws, voff, i * stride); }
#pragma unroll 1
  for (int it = 0; it < SPIN_CAP; ++it) {
    VM0();
    u32 bad = 0;
#pragma unroll
    for (int i = 0; i < N; ++i) { PIN(v[i]); x[i] = v[i] ^ tb; bad |= x[i]; }
    if (__builtin_amdgcn_ballot_w64((bad & 0xffu) != 0u) == 0) break;
#pragma unroll
    for (int i = 0; i < N; ++i) {
      const u64 m = __builtin_amdgcn_ballot_w64((x[i] & 0xffu) != 0u);
      ldm(v[i], ((u32)m ? 0xffffffffull : 0ull) | ((m >> 32) ? 0xffffffff00000000ull : 0ull), ws, voff, i * stride);
    }
  }
  VM0();
  return tree<0, N>(x);
}
template <int NKS> __device__ __forceinline__ void read_w64(LAS unsigned char* L, int wimg, int lane, u32x4 (&a)[4 * NKS]) {
  const int wa = wimg + (8 * (lane >> 4) + ((lane & 15) >> 2)) * 128 + (lane & 3) * 8;
#pragma unroll
  for (int i = 0; i < 4 * NKS; ++i) a[i] = tr_frag(L, wa + (i >> 2) * 4096 + (i & 3) * 32, 128);
  __builtin_amdgcn_sched_barrier(0);
}
template <int NKS> __device__ __forceinline__ void read_x64(LAS unsigned char* L, int ximg, int xstride, int lane, u32x4 (&x)[4 * NKS]) {
  const int xa = ximg + (lane & 1) * xstride + (lane >> 4) * 16;
#pragma unroll
  for (int i = 0; i < 4 * NKS; ++i) x[i] = *lp<u32x4>(L, (((lane & 15) >> 2) == (i & 3) ? xa : L_ZERO) + (i >> 2) * 64);
  __builtin_amdgcn_sched_barrier(0);
}
template <int N> __device__ __forceinline__ f32x4 mfma_chain(const u32x4* a, const u32x4* x) {
  f32x4 d[2] = {{0, 0, 0, 0}, {0, 0, 0, 0}};
#pragma unroll
  for (int i = 0; i < N; ++i) d[i & 1] = mfma(a[i], x[i], d[i & 1]);
  return d[0] + d[1];
}
__device__ __forceinline__ f32x2 rbf2(f32x2 v) { u32 w = pk2bf(v[0], v[1]); asm("" : "+v"(w)); return f32x2{bflo(w), bfhi(w)}; }
__device__ __forceinline__ f32x4 rbf4(f32x4 v) {
  u32 w0 = pk2bf(v[0], v[1]), w1 = pk2bf(v[2], v[3]);
  asm("" : "+v"(w0), "+v"(w1));
  return f32x4{bflo(w0), bfhi(w0), bflo(w1), bfhi(w1)};
}
__device__ __forceinline__ f32x4 up4(u32x2 w) { return f32x4{bflo(w[0]), bfhi(w[0]), bflo(w[1]), bfhi(w[1])}; }
__device__ __forceinline__ u32x2 hi4(f32x4 z) { return u32x2{__builtin_amdgcn_perm(fbits(z[1]), fbits(z[0]), 0x07060302u), __builtin_amdgcn_perm(fbits(z[3]), fbits(z[2]), 0x07060302u)}; }
__device__ __forceinline__ f32x2 up2(u32 w) { return f32x2{bflo(w), bfhi(w)}; }

template <int V> struct IC { static constexpr int v = V; };
template <int A, int B, typename F> __device__ __forceinline__ void sfor(F&& f) { if constexpr (A < B) { f(IC<A>{}); sfor<A + 1, B>(f); } }
#define INL __attribute__((always_inline))
constexpr int NPARK = NPARK_;

typedef u64 u64x8 __attribute__((ext_vector_type(8)));
typedef u64 u64x4 __attribute__((ext_vector_type(4)));
#define PINS(x) asm volatile("" : "+s"(x))
__global__ void __launch_bounds__(512, 2) fused_block(Params p_) {
  const int wave = __builtin_amdgcn_readfirstlane(threadIdx.x >> 6);
  const int xg = blockIdx.x & 7, r32 = blockIdx.x >> 3;
  u64x8 ka, kf; u64 job, tnow;
  asm volatile("s_load_dwordx2 %1, %4, %5\n\ts_load_dwordx16 %3, %4, 0x0\n\ts_load_dwordx16 %0, %4, 0x40\n\ts_memrealtime %2"
               : "=&s"(ka), "=&s"(job), "=&s"(tnow), "=&s"(kf) : "s"(__builtin_amdgcn_kernarg_segment_ptr()), "s"(0x80 + (r32 * 8 + wave) * 8));
  u32x4 pf;
  asm volatile("s_cmp_lg_u32 %1, 5\n\ts_cbranch_scc1 1f\n\ts_getpc_b64 vcc\n\tglobal_load_dwordx4 %0, %2, vcc\n1:"
               : "=&v"(pf) : "s"(wave), "v"(min((blockIdx.x >> 3) * 1024 + (threadIdx.x & 63) * 16, CODEPF - 16u)) : "memory", "scc", "vcc");
  __shared__ __attribute__((aligned(16))) unsigned char smem[L_END];
  LAS unsigned char* L = (LAS unsigned char*)smem;
  volatile LAS u32* FL = (volatile LAS u32*)(L + L_FLAG);
  const int lb = (int)(u32)(size_t)L;
  const int tid = threadIdx.x, lane = tid & 63;
  const int g = lane >> 4, c = lane & 15;
  const int wg = xg * 32 + r32, kvh = wg >> 4, b = (wg >> 3) & 1;
  constexpr int nt = 8;
  const bool is_wo = r32 < 9, is_mlp = r32 >= 9 && r32 < 27;
  const int mu = (r32 < 12 ? r32 : r32 < 23 ? 31 : r32 - 11) * 8 + xg;
  const int lam = mu * 171 >> 9, kap = mu - 3 * lam;
  const bool has = mu < 162;
  if (wave == 5) {
    *lp<u32x2>(L, L_ZERO + lane * 8) = u32x2{0, 0};
    FL[lane] = 0u;
  }
  int som = kap * 192 * NMOD * 2 + lam * 128, xs0 = wave == 6 ? kap * 384 : 0, xo = ((lane >> 5) * 288 + (lane & 31)) * 4;
  PIN(xo);
  asm volatile("s_waitcnt lgkmcnt(0)\n\ts_barrier" : "+s"(ka), "+s"(kf), "+s"(job), "+s"(tnow), "+s"(som), "+s"(xs0) :: "memory");
  typedef const u16 __attribute__((address_space(1)))* gptr;
  const struct { u64 kc, vc; gptr b1; u64 w2; gptr b2; u64 out, ws, w1; } p = {ka[0], ka[1], (gptr)ka[2], ka[3], (gptr)ka[4], ka[5], ka[6], ka[7]};
  const u32 tstart = (u32)tnow;
  const rsrc_t rs0 = mkrs(wave == 6 ? kf[6] : kf[7], 2 * D * 2), ws = mkrs(p.ws, W_END);
  u32 v9[9];
  u32x2 ep;
  asm volatile("s_cmp_lt_u32 %[w], 6\n\ts_cbranch_scc1 2f\n\t"
               "buffer_load_dword %0, %[vo], %[rs], %[so] offen\n\tbuffer_load_dword %1, %[vo], %[rs], %[so] offen offset:128\n\t"
               "buffer_load_dword %2, %[vo], %[rs], %[so] offen offset:256\n\tbuffer_load_dwordx2 %9, off, %[ws], 0 sc1\n\t"
               "buffer_load_dword %3, %[vo], %[rs], %[so] offen offset:384\n\tbuffer_load_dword %4, %[vo], %[rs], %[so] offen offset:512\n\t"
               "buffer_load_dword %5, %[vo], %[rs], %[so] offen offset:640\n\tbuffer_load_dword %6, %[vo], %[rs], %[so] offen offset:768\n\t"
               "buffer_load_dword %7, %[vo], %[rs], %[so] offen offset:896\n\tbuffer_load_dword %8, %[vo], %[rs], %[so] offen offset:1024\n2:"
               : "=&v"(v9[0]), "=&v"(v9[1]), "=&v"(v9[2]), "=&v"(v9[3]), "=&v"(v9[4]), "=&v"(v9[5]), "=&v"(v9[6]), "=&v"(v9[7]), "=&v"(v9[8]), "=&v"(ep)
               : [w] "s"(wave), [vo] "v"(xo), [rs] "s"(rs0), [so] "s"(xs0), [ws] "s"(ws) : "memory", "scc");
  const int mrot = r32 - 9 + 2 * (((3 - (int)(p.w1 >> 7) - xg) & 7) + 3), mlp = xg * 18 + (mrot >= 36 ? mrot - 36 : mrot >= 18 ? mrot - 18 : mrot);
  {
    const u32 j1 = (u32)(job >> 32), kind = (j1 >> 3) & 3;
    u64 pa = (kind & 1) ? kf[1] : kf[0], pb = (kind & 1) ? kf[3] : kf[2];
    const int rowb = (0x4840092bu >> (8 * kind) & 0xff) * 128 + (kind == 0 ? 1408 : 0), sh = kind == 3 ? 2 : 3;
    int sa = (kind & 1) ? xg * 512 * D * 2 : som, sb = (kind & 1) ? mlp * 64 : xg * 1024, n0 = kind == 0 && !has ? 0 : (j1 >> 8) & 15;
    PINS(pa); PINS(pb); PINS(sa); PINS(sb);
    if (j1 >> 20) lds_wait(L, F_CI);
    dma_rows(mkrs((kind & 2) ? pb : pa, (kind == 1 ? HQ * DH : D) * rowb), lb + ((j1 >> 12) & 0xff) * 1024, (lane >> sh) * rowb + (lane & ((1 << sh) - 1)) * 16,
             (u32)job + ((kind & 2) ? sb : sa), rowb << (6 - sh), n0);
    if (wave == 4) { asm volatile("s_waitcnt vmcnt(%0)" :: "n"(CRITVM) : "memory"); FL[F_CI] = 1u; }
  }
  __builtin_amdgcn_sched_barrier(0);
  const int red = (r32 - 27) * 8 + xg;
  const bool is_red = red >= 0 && red < 36;
  u32x4 wf[16];
#define DG(i)

  if (__builtin_expect(wave < 4, 1)) {
    const rsrc_t krs = mkrs(p.kc, 2 * HKV * SK * DH * 2);
    const int base = ((b * HKV + kvh) * SK + (wg & 7) * 1024) * 256 + wave * 4096, vo1 = (c >> 2) * 256 + g * 64 + (c & 3) * 16;
    u32x4 ring[56];
    constexpr u32 c0 = (C0X2 << KSH) / 2;
    u32 slope = is_wo ? (TEND - WOAHEAD - PLAN0) * 64 / 127 : (TEND - PLAN0) * 64 / 127;
    u32 ksh = KSH + 6, shifted = 0;
    u32 due = (tstart + PLAN0 - c0) * 64;
    PINS(slope); PINS(ksh); PINS(due);
    if (KGO) pause_until(tstart + KGO);
    auto replan = [&]() INL {
      const u32 o = __builtin_amdgcn_readfirstlane(FL[F_ORG]) * 64;
      FL[F_SMP] = (tstart - c0 + ((int)(clock32() * 64 - (due - slope) + shifted) >> 6)) | 1u;
      due += o - shifted; shifted = o;
    };
    auto ldk = [&](auto N) INL {
      constexpr int n = decltype(N)::v;
      __builtin_amdgcn_sched_barrier(0);
      gate(due, slope, ksh);
      ring[n] = __builtin_amdgcn_raw_buffer_load_b128(krs, vo1 + (n & 3) * 1024, base + (n >> 2) * 16384, AUX_NT);
      if (ORG && n % 8 == 4 && n > 16 && n < NPARK - 5) replan();
      if (n == 8 && !is_wo) DG(5);
      if (ADAPT && n == 8 && wave == 0) FL[F_ORG] = max(((int)(clock32() * 64 - due) >> 6) - (int)c0 - ADAPT, 0);
      if (ADAPT && n == 10) replan();
      if (n == 8) FL[F_WQ + wave] = clock32() | 1u;
      if (n == NPARK - QLEAD) FL[F_NEAR] = 1u;
      if (n == NPARK - 5) due -= BOOST * 64;
      if (n == NPARK - 1) due += BOOST * 64;
      __builtin_amdgcn_sched_barrier(0);
    };
    sfor<0, NPARK>(ldk);
    STAMP(1);
    lds_wait(L, F_Q);
    STAMP(2);
    const int qa = L_Q + (((c >> 2) & 1) * 128 + 32 * g + 8 * (c & 3)) * 2;
    const u32x4 a1 = *lp<u32x4>(L, c < 8 ? qa : L_ZERO), a2 = *lp<u32x4>(L, c < 8 ? L_ZERO : qa);
    u32x4 nx[8];
    u64 nptr; int nvo, nso, nsz;
    auto next_tile = [&](int t) INL {
      const bool k = t < nt;
      nptr = k ? p.kc : p.vc; nvo = k ? vo1 : lane * 16; nso = base + (k ? t : t - nt) * 32768; nsz = t < 2 * nt ? 2 * HKV * SK * DH * 2 : 0;
    };
    auto ldn = [&](int i) INL {
      __builtin_amdgcn_sched_barrier(0);
      gate(due, slope, ksh);
      nx[i] = __builtin_amdgcn_raw_buffer_load_b128(mkrs(nptr, nsz), nvo + (i & 3) * 1024, nso + (i >> 2) * 16384, AUX_NT);
      __builtin_amdgcn_sched_barrier(0);
    };
    const int pw = L_P + wave * 2304 + (((c >> 2) * 2 + (g & 1)) * 8 + 2 * (c & 3) + (g >> 1)) * 4;
    float m = -INFINITY;
    auto scores = [&](const f32x4 (&d)[4], int T) INL {
      float xs[4];
#pragma unroll
      for (int u = 0; u < 4; ++u) { xs[u] = ((dpp<0x00>(d[u][0]) + dpp<0x55>(d[u][1])) + dpp<0xAA>(d[u][2])) + dpp<0xFF>(d[u][3]); PIN(xs[u]); }
      const float x01 = (c & 1) ? xs[1] : xs[0], x23 = (c & 1) ? xs[3] : xs[2];
      const float sc = rbf(rbf((c & 2) ? x23 : x01) * 0.08838834764831845f);
      m = fmaxf(m, sc);
      *lp<float>(L, pw + T * 256) = sc;
      __builtin_amdgcn_sched_barrier(0);
    };
    next_tile(7);
    sfor<0, 7>([&](auto TT) INL {
      constexpr int T = decltype(TT)::v;
      f32x4 d[4];
      sfor<0, 4>([&](auto U) INL {
        constexpr int u = decltype(U)::v, n = 8 * T + 2 * u;
        d[u] = mfma(a1, ring[n], f32x4{0, 0, 0, 0});
        d[u] = mfma(a2, ring[n + 1], d[u]);
        constexpr int R = 64 - NPARK, k0 = T == 0 ? (u * (R - 6 > 4 ? 4 : 2) + 3) / 4 : (R > 8 ? 4 : 2) + ((4 * (T - 1) + u) * (R - (R > 8 ? 4 : 2)) + 23) / 24;
        constexpr int k1 = T == 0 ? ((u + 1) * (R - 6 > 4 ? 4 : 2) + 3) / 4 : (R > 8 ? 4 : 2) + ((4 * (T - 1) + u + 1) * (R - (R > 8 ? 4 : 2)) + 23) / 24;
        if constexpr (k1 > k0) {
          if constexpr (NPARK + k0 < 56) ldk(IC<NPARK + k0>{}); else ldn(NPARK + k0 - 56);
          if (NPARK + k0 == 60) { if (ORG) replan(); due -= nt == 7 ? BOOST * 64 : 0; }
        }
        else __builtin_amdgcn_sched_barrier(0);
      });
      scores(d, T);
    });
#pragma unroll 1
    for (int T = 7; T < nt; ++T) {
      next_tile(T + 1);
      f32x4 d[4];
#pragma unroll
      for (int u = 0; u < 4; ++u) {
        d[u] = mfma(a1, nx[2 * u], f32x4{0, 0, 0, 0});
        if (u == 2) due -= T + 1 == nt ? BOOST * 64 : 0;
        ldn(2 * u);
        d[u] = mfma(a2, nx[2 * u + 1], d[u]);
        ldn(2 * u + 1);
      }
      scores(d, T);
    }
    due += BOOST * 64;
    STAMP(3);
    float sc[9];
#pragma unroll
    for (int T = 0; T < 9; ++T) sc[T] = *lp<float>(L, pw + T * 256);
    m = x32_max(row_max(m));
    float l = 0.f;
#pragma unroll
    for (int T = 0; T < 9; ++T) {
      const float e = (T < 7 || T < nt) ? __builtin_amdgcn_exp2f((sc[T] - m) * LOG2E) : 0.f;
      l += e;
      *lp<u32>(L, pw + T * 256) = pk2bf(e, e);
    }
    l = x32_sum(row_sum(l));
    if (c == 0 && g < 2) *lp<f32x2>(L, L_ML + wave * 16 + g * 8) = f32x2{m, l};
    lds_set(L, F_ML + wave);
    u32 mk[4];
#pragma unroll
    for (int k = 0; k < 4; ++k) mk[k] = ((c & 7) >> 1) == k ? ((c & 1) ? 0xffff0000u : 0xffffu) : 0u;
    f32x4 acc = {0, 0, 0, 0};
    int pr = L_P + wave * 2304 + (g * 2 + (c >> 3)) * 32;
#pragma unroll 1
    for (int T = 1; T < nt; ++T) {
      next_tile(nt + T);
      const u32x4 p0 = *lp<u32x4>(L, pr), p1 = *lp<u32x4>(L, pr + 16);
      pr += 256;
      if (T == 5) FL[F_V] = 1u;
      if (T == ENDT) slope += ENDSLACK;
#pragma unroll
      for (int i = 0; i < 8; ++i) {
        const u32 pv = i < 4 ? p0[i & 3] : p1[i & 3];
        acc = mfma(u32x4{pv & mk[0], pv & mk[1], pv & mk[2], pv & mk[3]}, nx[i], acc);
        ldn(i);
      }
      if (ORG) replan();
    }
    {
      const u32 now = clock32();
      FL[F_LAST] = (now + ((int)(due - slope - now * 64) >> 6) + c0 + (is_wo ? WOAHEAD : 0)) | 1u;
    }
    STAMP(4);
    int t1a = W_T1 + wg * 1024 + lane * 16, aw = lb + L_ACC + wave * 1024 + lane * 16, ar = lb + L_ACC + lane * 16, ca = lb + L_FLAG + F_CNT * 4, one = 1;
    lds_wait(L, F_SC);
    float fs = *lp<float>(L, L_SC + wave * 8 + (g >> 1) * 4);
    {
      const u32x4 p0 = *lp<u32x4>(L, pr), p1 = *lp<u32x4>(L, pr + 16);
      u32x4 af[8];
#pragma unroll
      for (int i = 0; i < 8; ++i) { const u32 pv = i < 4 ? p0[i & 3] : p1[i & 3]; af[i] = u32x4{pv & mk[0], pv & mk[1], pv & mk[2], pv & mk[3]}; PIN(af[i]); }
      PIN(fs); PIN(t1a); PIN(aw); PIN(ar); PIN(ca); PIN(one);
#pragma unroll
      for (int i = 0; i < 8; ++i) acc = mfma(af[i], nx[i], acc);
    }
    acc *= fs;
    u32 arr;
    f32x4 r0, r1, r2, r3;
    asm volatile("ds_write_b128 %5, %6\n\ts_mov_b64 exec, 1\n\tds_add_rtn_u32 %0, %7, %8\n\ts_mov_b64 exec, -1\n\tds_read_b128 %1, %9\n\tds_read_b128 %2, %9 offset:1024\n\t"
                 "ds_read_b128 %3, %9 offset:2048\n\tds_read_b128 %4, %9 offset:3072\n\ts_waitcnt lgkmcnt(0)"
                 : "=&v"(arr), "=&v"(r0), "=&v"(r1), "=&v"(r2), "=&v"(r3) : "v"(aw), "v"(acc), "v"(ca), "v"(one), "v"(ar) : "memory");
    STAMP(5);
    if (__builtin_amdgcn_readfirstlane(arr) == 3u) {
      stsc(nosent((r0 + r1) + (r2 + r3)), ws, t1a);
      FL[F_PUB] = clock32() | 1u;
      if (!is_wo) DG(3);
      STAMP(7);
      NOTE(13, (tnow & ~0xffffffffull) | FL[F_LAST]);
    }
  } else {
    __builtin_amdgcn_s_setprio(3);
    u32 (&v)[9] = v9;
    asm volatile("s_cmp_eq_u32 %[w], 4\n\ts_cbranch_scc1 1f\n\ts_waitcnt vmcnt(0)\n1:"
                 : "+v"(v[0]), "+v"(v[1]), "+v"(v[2]), "+v"(v[3]), "+v"(v[4]), "+v"(v[5]), "+v"(v[6]), "+v"(v[7]), "+v"(v[8]), "+v"(ep), "+v"(pf)
                 : [w] "s"(wave) : "memory", "scc");
    if (__builtin_expect(wave == 4, 1)) {
    lds_wait(L, F_SI);
    const u32x2 tv = *lp<u32x2>(L, L_TAG);
    const Tag tg = {tv[0], tv[1]};
    if (has) {
      int sa = (c & 2) ? W_END : W_MP + (kap * 216 + lam * 4 + (c >> 2)) * 128 + (c & 1) * 64 + g * 16;
      u32x4 tn = tag_nibs(tg, g);
      PIN(sa); PIN(tn);
      u32x4 a[24], x[24];
      read_x64<6>(L, L_SILU, 384, lane, x);
      VM0();
      STAMP(1);
      read_w64<6>(L, L_WM, lane, a);
      const f32x4 d = mfma_chain<24>(a, x);
      stsc(put_nibs(d, tn), ws, sa);
      if (mu < 54) DG(1);
      STAMP(2);
    }
    if (r32 >= 12 && r32 < 21) { VM0(); FL[F_WS] = 1u; }
    const u32x4 e4 = {SENT, SENT, SENT, SENT};
    if (!(XA & 4)) stsc(e4, ws, W_T1 + wg * 1024 + lane * 16);
    if (is_wo && lane < 32) stsc(e4, ws, W_T2 + (xg * 36 + r32 * 4) * 128 + lane * 16);
    if (has && kap == 0 && lam >= 18) {
      f32x4 v[3];
      lds_wait(L, F_WQ);
      pause_until(__builtin_amdgcn_readfirstlane(FL[F_WQ]) + FDELAY);
      gather_nibs<3>(ws, [&](int i, int& vo, int& so) { vo = W_MP + lam * 512 + (lane & 31) * 16; so = i * 216 * 128; }, tag_nibs(tg, lane & 3), v, [] {});
      const f32x4 t = (v[0] + v[1]) + v[2];
      if (lane < 32) stsc(u32x2{pk2bf(t[0], t[1]), pk2bf(t[2], t[3])}, ws, W_MODF + (((lane >> 2) & 1) * 2304 + (lam - 18) * 64 + 16 * (lane >> 3) + 4 * (lane & 3)) * 2);
    }
    VM0();
    if (lane == 0) stsc(u32x2{tg.a, tg.b}, ws, W_RDY + wg * 8);
    lds_set(L, F_CLR);
    STAMP(3);
    } else {
      if (wave == 6) {
#pragma unroll
      for (int i = 0; i < 3; ++i) {
        const float v0 = bflo(v[i]), v1 = bfhi(v[i]);
        *lp<u32>(L, L_SILU + (lane >> 5) * 384 + ((lane & 31) + 32 * i) * 4) = pk2bf(v0 * rcp(1.f + __expf(-v0)), v1 * rcp(1.f + __expf(-v1)));
      }
      const Tag tg = make_tag(ep);
      *lp<u32x2>(L, L_TAG) = u32x2{tg.a, tg.b};
      lds_set(L, F_SI);
      DG(7);
      STAMP(1);
      } else if (wave == 7) {
      const int xi = xo >> 2;
      f32x2 sum = {0, 0};
#pragma unroll
      for (int i = 0; i < 9; ++i) sum += up2(v[i]);
      const float mean = x16_sum(row_sum(sum[0] + sum[1])) * (1.f / D);
      f32x2 sq = {0, 0};
#pragma unroll
      for (int i = 0; i < 9; ++i) { const f32x2 dd = up2(v[i]) - mean; sq += dd * dd; }
      const float mb = rbf(mean), ri = rcp(rbf(__builtin_amdgcn_sqrtf(rbf(rbf(x16_sum(row_sum(sq[0] + sq[1])) * (1.f / D)) + 1e-5f))));
#pragma unroll
      for (int i = 0; i < 9; ++i) {
        *lp<f32x2>(L, L_LNX + (xi + 32 * i) * 8) = rbf2(rbf2(up2(v[i]) - mb) * ri);
        *lp<u32>(L, L_XB + (xi + 32 * i) * 4) = v[i];
      }
      *lp<float>(L, L_MB + (lane >> 5) * 4) = mb;
      lds_set(L, F_LN);
      STAMP(1);
      }
      if (TLBG && wave == 5) {
        u32 t0, t1, t2, t3;
        const u32 ok = ((b * HKV + kvh) * SK + (wg & 7) * 1024) * 256;
        if (TLBG > 1) pause_until(tstart + TLBG);
        asm volatile("s_load_dword %0, %4, %6\n\ts_load_dword %1, %5, %6\n\ts_load_dword %2, %7, %8\n\ts_load_dword %3, %9, 0x0\n\ts_waitcnt lgkmcnt(0)"
                     : "=&s"(t0), "=&s"(t1), "=&s"(t2), "=&s"(t3) : "s"(p.kc), "s"(p.vc), "s"(ok), "s"(p.w2), "s"(wg * 20736), "s"(p.out) : "memory");
      }
      lds_wait(L, F_SI);
      const u32x2 tv = *lp<u32x2>(L, L_TAG);
      const Tag tg = {tv[0], tv[1]};
      const int sg = r32 - 12, sj = sg * 11 >> 5;
      const bool spare = r32 >= 27;
      if (spare || (sg >= 0 && sg < 9 && sg - 3 * sj == wave - 5)) {
        const int w3 = wave - 5, l48 = min(lane, 47), bq = (l48 >> 2) & 1, d0 = 16 * (l48 >> 3) + 4 * (l48 & 3);
        const int qc = ((spare ? r32 - 27 : 5 + sj) + 3 * w3) & 7;
        int sa = (c & 2) ? W_END : W_QF + ((xg * 2 + (qc >> 2)) * 2 + (c & 1)) * 3072 + w3 * 1024 + ((qc & 3) * 4 + (c >> 2)) * 64 + g * 16;
        int hw = L_HS + w3 * 768 + bq * 384 + d0 * 2;
        u32x4 tn = tag_nibs(tg, g);
        lds_wait(L, F_LN);
        const f32x4 ln[2] = {*lp<f32x4>(L, L_LNX + (bq * D + 192 * w3 + d0) * 4), *lp<f32x4>(L, L_LNX + (bq * D + 192 * w3 + d0 + 96) * 4)};
        f32x4 mv[12];
        u32x4 a[24], x[24];
        PIN(sa); PIN(tn); PIN(hw);
        if (ADAPT) { lds_wait(L, F_WQ); pause_until(__builtin_amdgcn_readfirstlane(FL[F_WQ]) + HAFTER); }
        pause_until(tstart + HDELAY);
        STAMP(11);
        const int nr = gather_nibs<12>(ws, [&](int i, int& vo, int& so) { vo = W_MP + w3 * 1536 + l48 * 16 + (i & 1) * 768; so = ((i >> 2) * 216 + ((i >> 1) & 1) * 36) * 128; },
                                       tag_nibs(tg, l48 & 3), mv, [&]() INL { lds_wait(L, spare ? F_WQ + w3 : F_WS); read_w64<6>(L, spare ? L_WT + w3 * 24576 : L_WM, lane, a); });
        STAMP(7);
        NOTE(13, nr + 1);
#pragma unroll
        for (int i = 0; i < 2; ++i) {
          const f32x4 h = rbf4(ln[i] * rbf4(1.f + rbf4((mv[2 + i] + mv[6 + i]) + mv[10 + i]))) + rbf4((mv[i] + mv[4 + i]) + mv[8 + i]);
          *lp<u32x2>(L, hw + 192 * i) = u32x2{pk2bf(h[0], h[1]), pk2bf(h[2], h[3])};
        }
        read_x64<6>(L, L_HS + w3 * 768, 384, lane, x);
        stsc(put_nibs(mfma_chain<24>(a, x), tn), ws, sa);
        DG(6);
        NOTE(10, __builtin_amdgcn_s_memrealtime());
      }
      if (wave == 6) {
      int org = 0;
      if (ADAPT) { lds_wait(L, F_WQ); org = __builtin_amdgcn_readfirstlane(FL[F_ORG]); }
      auto exchange = [&](int k) INL {
        const u32 t = tstart + org + ORGT0 + k * ORGDT;
        pause_until(t);
        const u32 smp = FL[F_SMP];
        if ((r32 & 7) == 5 && smp && lane < 8) stsc(u32x4{smp, tg.a, 0u, tg.b}, ws, W_ORG_ + k * 4096 + lane * 512 + (xg * 4 + (r32 >> 3)) * 16);
        pause_until(t + (k == 0 ? ORGLAG0 : ORGLAG));
        u32x4 sv;
        ldsc(sv, ws, W_ORG_ + k * 4096 + xg * 512 + (lane & 31) * 16, 0);
        const bool ok = sv[1] == tg.a && sv[3] == tg.b;
        float dt = ok ? fminf(fmaxf((float)(int)(sv[0] - tstart - org), -200.f), 200.f) : 0.f, cnt = ok ? 1.f : 0.f;
        dt += dpp<XOR1>(dt); cnt += dpp<XOR1>(cnt); dt += dpp<XOR2>(dt); cnt += dpp<XOR2>(cnt);
        float gm = cnt > 0.f ? dt * rcp(cnt) : -1000.f;
        gm = fmaxf(gm, dpp<XOR8>(gm)); gm = fmaxf(gm, dpp<ROR4>(gm));
        { auto u = __builtin_amdgcn_permlane16_swap(fbits(gm), fbits(gm), false, false); gm = fmaxf(bitsf(u[0]), bitsf(u[1])); }
        dt += dpp<XOR8>(dt); cnt += dpp<XOR8>(cnt); dt += dpp<ROR4>(dt); cnt += dpp<ROR4>(cnt);
        dt = x16_sum(dt); cnt = x16_sum(cnt);
        const float mean = dt * rcp(fmaxf(cnt, 1.f));
        org += __builtin_amdgcn_readfirstlane((int)(mean + (k == 0 ? BETA0 : BETA) * 0.125f * fmaxf(gm - mean, 0.f))) + ORGBIAS;
        FL[F_ORG] = org;
      };
#pragma unroll 1
      for (int k = 0; k < NORG; ++k) {
      exchange(k);
      if (k != 1) continue;
#pragma unroll 1
      for (int it = 0; it < SPIN_CAP; ++it) {
        asm volatile("" ::: "memory");
        u32x4 r0, r1;
        ldsc(r0, ws, W_RDY + lane * 16, 0); ldsc(r1, ws, W_RDY + lane * 16, 1024);
        const u32 bad = (r0[0] ^ tg.a) | (r0[1] ^ tg.b) | (r0[2] ^ tg.a) | (r0[3] ^ tg.b) | (r1[0] ^ tg.a) | (r1[1] ^ tg.b) | (r1[2] ^ tg.a) | (r1[3] ^ tg.b);
        if (!__any(bad != 0u)) break;
        __builtin_amdgcn_s_sleep(32);
      }
      asm volatile("" ::: "memory");
      STAMP(2);
      if (!is_wo && !(XA & 8)) {
        u32x4 mv[9];
#pragma unroll
        for (int i = 0; i < 9; ++i) ldsc(mv[i], ws, W_MODF + lane * 16, i * 1024);
        const u32 bias = (is_mlp ? p.b1 + mlp * 32 : p.b2 + min(red, 35) * 16)[lane & (is_mlp ? 31 : 15)];
#pragma unroll
        for (int i = 0; i < 9; ++i) *lp<u32x4>(L, L_MODF + i * 1024 + lane * 16) = mv[i];
        *lp<float>(L, L_BIAS + lane * 4) = bflo(bias);
      }
      STAMP(3);
      }
      } else if (wave == 5) {
    {
      f32x4 qv[3];
      lds_wait(L, F_NEAR);
      STAMP(12);
      const int nr = gather_nibs<3>(ws, [&](int i, int& vo, int& so) { vo = W_QF + (kvh * 2 + b) * 3072 + lane * 16; so = i * 1024; }, tag_nibs(tg, lane & 3), qv, [] {});
      NOTE(14, nr + 1);
      const f32x4 t = (qv[0] + qv[1]) + qv[2];
      *lp<u32x2>(L, L_Q + lane * 8) = u32x2{pk2bf(t[0], t[1]), pk2bf(t[2], t[3])};
    }
    lds_set(L, F_Q);
    DG(2);
    STAMP(4);
    lds_wait(L, F_ML, 4);
    const f32x2 ml = *lp<f32x2>(L, L_ML + (lane & 7) * 8);
    float M = fmaxf(ml[0], dpp<XOR2>(ml[0])); M = fmaxf(M, dpp<ROR4>(M));
    float Ls = ml[1] * __builtin_amdgcn_exp2f((ml[0] - M) * LOG2E); Ls += dpp<XOR2>(Ls); Ls += dpp<ROR4>(Ls);
    if (lane < 2) stsc(u32x4{fbits(M), tg.a, fbits(Ls), tg.b}, ws, W_ML + wg * 128 + lane * 16);
    u32x4 gv[1];
    lds_wait(L, F_V);
    gather_tag<1>(ws, [&](int i, int& vo, int& so) { vo = W_ML + ((wg & ~7) + (c >> 1)) * 128 + (c & 1) * 16; so = 0; }, tg, gv);
    const float ms = bitsf(gv[0][0]);
    float Mg = fmaxf(ms, dpp<XOR2>(ms)); Mg = fmaxf(Mg, dpp<ROR4>(Mg)); Mg = fmaxf(Mg, dpp<XOR8>(Mg));
    float Lg = bitsf(gv[0][2]) * __builtin_amdgcn_exp2f((ms - Mg) * LOG2E); Lg += dpp<XOR2>(Lg); Lg += dpp<XOR8>(Lg); Lg += dpp<ROR4>(Lg);
    if (lane < 8) *lp<float>(L, L_SC + lane * 4) = __builtin_amdgcn_exp2f((ml[0] - Mg) * LOG2E) / Lg;
    lds_wait(L, F_CLR);
    lds_set(L, F_SC);
    STAMP(5);
      }
    }
  }

#define CY(i)
  if (is_wo) {
    if (wave >= 4) {
      if (wave == 6) lds_set(L, F_RDY);
      lds_wait(L, F_WQ, 4);
      const int w4 = wave - 4, wa = L_WT + (8 * g + (c >> 2)) * 128 + w4 * 32 + (c & 3) * 8;
#pragma unroll
      for (int ks = 0; ks < 16; ++ks) wf[ks] = tr_frag(L, wa + ks * 4096, 128);
#pragma unroll
      for (int ks = 0; ks < 16; ++ks) PIN(wf[ks]);
      int oa = L_O + (w4 & 1) * 1056 + ((w4 >> 1) * 256 + (g >> 1) * 128 + 8 * c + 4 * (g & 1)) * 2, xa = L_O + (c & 1) * 1056 + g * 16;
    {
        int sa = c < 2 ? W_T2 + (xg * 36 + r32 * 4 + w4) * 128 + c * 64 + g * 16 : W_END;
        int ga = W_T1 + (xg * 32 + w4 * 8) * 1024 + lane * 16;
        PIN(oa); PIN(xa); PIN(sa); PIN(ga);
        {
          lds_wait(L, F_RDY);
          lds_wait(L, F_PUB);
          STAMP(6);
          pause_until(__builtin_amdgcn_readfirstlane(FL[F_PUB]) + T1DELAY);
          pause_until(__builtin_amdgcn_readfirstlane(FL[F_LAST]) + T1COMMON);
        }
        const f32x4 o = gather_sum<8, u32x4>(ws, ga, 1024, [] {});
        STAMP(8);
        *lp<u32x2>(L, oa) = u32x2{pk2bf(o[0], o[1]), pk2bf(o[2], o[3])};
        BAR();
        u32x4 x[16];
#pragma unroll
        for (int ks = 0; ks < 16; ++ks) x[ks] = *lp<u32x4>(L, xa + ks * 64);
        __builtin_amdgcn_sched_barrier(0);
        stsc(nosent(mfma_chain<16>(wf, x)), ws, sa);
        STAMP(9);
      }
    }
  } else if (is_mlp) {
    if (wave == 6) {
      const rsrc_t rs = mkrs(p.w2, D * FF * 2);
      lds_wait(L, F_LAST);
      dma_rows(rs, lb + L_WT + 36864, lane * 16, mlp * 36864, 1024, W2PRE / 4);
      lds_wait(L, F_PUB);
      dma_rows(rs, lb + L_WT + 36864 + W2PRE * 1024, lane * 16, mlp * 36864 + W2PRE * 1024, 1024, (36 - W2PRE) / 4);
    }
    const int tl = wave >= 3, th = wave - 3 * tl;
    const int ln = min(lane, 47), bb = (ln >> 2) & 1, col = 96 * wave + 16 * (ln >> 3) + 4 * (ln & 3);
    const int b2 = c & 1, n2 = 8 * g + (c >> 1);
    __builtin_amdgcn_s_setprio(3);
    BAR();
    STAMP(6);
    if (wave == 6) { VM0(); STAMP(12); lds_set(L, F_W); }
    float bias = *lp<float>(L, L_BIAS + n2 * 4);
    int ha = L_H1P + n2 * 32 + g * 32 + b2 * 16, sta = L_ST + b2 * 8;
    int sa3 = c < (wave < 4 ? 10 : 8) ? W_T3 + (wave + 8 * (c >> 1)) * T3_RED + mlp * 128 + b2 * 64 + g * 16 : W_END;
    u32 psel = (c & 2) ? 0x05040100u : 0x01000504u;
    const u32x2 tv = *lp<u32x2>(L, L_TAG);
    u32x4 tb = tag_bytes(Tag{tv[0], tv[1]}, g & 1);
    int wa2 = L_WT + 36864 + (8 * g + (c >> 2)) * 1152 + (c & 3) * 8 + wave * 32;
    PIN(bias); PIN(ha); PIN(sta); PIN(sa3); PIN(tb); PIN(psel); PIN(wa2);
    auto frags2 = [&]() INL {
      lds_wait(L, F_W);
#pragma unroll
      for (int i = 0; i < 5; ++i) {
        const u32x4 t = tr_frag(L, wa2 + i * 256, 1152);
        wf[6 + i] = u32x4{t[(i >> 1) & 3], t[((i >> 1) + 1) & 3], t[((i >> 1) + 2) & 3], t[((i >> 1) + 3) & 3]};
      }
#pragma unroll
      for (int i = 0; i < 5; ++i) PIN(wf[6 + i]);
      STAMP(10);
    };
    if (wave < 6) {
      const u32x2 gav = *lp<u32x2>(L, L_MODF + (bb * 2304 + col) * 2), sbv = *lp<u32x2>(L, L_MODF + (bb * 2304 + D + col) * 2),
                  cbv = *lp<u32x2>(L, L_MODF + (bb * 2304 + 2 * D + col) * 2), xv = *lp<u32x2>(L, L_XB + (bb * D + col) * 2);
#pragma unroll
      for (int ks = 0; ks < 6; ++ks) wf[ks] = tr_frag(L, L_WT + (192 * th + 8 * g + (c >> 2)) * 64 + tl * 32 + (c & 3) * 8 + ks * 2048, 64);
      const u32x2 gv = {pk2bf(1.f + bflo(cbv[0]), 1.f + bfhi(cbv[0])), pk2bf(1.f + bflo(cbv[1]), 1.f + bfhi(cbv[1]))};
      f32x4 ga = up4(gav), xx = up4(xv), gg = up4(gv);
      int za = L_XI + bb * XI_STR + col * 2, xa = L_XI + (c & 7) * XI_STR + th * 384 + g * 16, xw = L_X1 + (bb * D + col) * 4;
      int pa = L_H1P + th * 1152 + (tl * 16 + 4 * g) * 32 + (tl * 2 + (g >> 1)) * 32 + (c & 1) * 16 + ((c >> 1) & 3) * 4 + (c >> 3) * 64;
      int g2 = W_T2 + wave * 768 + ln * 16, x1a = mlp == 0 && lane < 48 ? W_T3 + (6 * wave + (ln >> 3)) * T3_RED + 144 * 128 + (ln & 7) * 16 : W_END;
      u32x4 tx = tag_bytes(Tag{tv[0], tv[1]}, ln & 1);
      const u32 go = max(__builtin_amdgcn_readfirstlane(FL[F_PUB]) + T2DELAY - tstart, __builtin_amdgcn_readfirstlane(FL[F_LAST]) + T2COMMON - tstart);
      *lp<u32x2>(L, za + 4 * XI_STR) = gv;
      *lp<u32x2>(L, za + 6 * XI_STR) = sbv;
      PIN(ga); PIN(xx); PIN(gg); PIN(za); PIN(xa); PIN(xw); PIN(pa); PIN(g2); PIN(x1a); PIN(tx);
#pragma unroll
      for (int i = 0; i < 6; ++i) PIN(wf[i]);
      STAMP(11);
      pause_until(tstart + go);
      const f32x4 a = gather_sum<8, u32x4>(ws, g2, 36 * 128, frags2);
      STAMP(8);
      CY(0);
      const f32x4 x1 = rbf4(xx + rbf4(ga * rbf4(a))), z = x1 * gg;
      *lp<f32x4>(L, xw) = x1;
      *lp<u32x2>(L, za) = hi4(z);
      *lp<u32x2>(L, za + 2 * XI_STR) = hi4(z - asf(__builtin_bit_cast(u32x4, z) & 0xffff0000u));
      CY(1);
      BAR();
      CY(2);
      u32x4 x[6];
#pragma unroll
      for (int ks = 0; ks < 6; ++ks) x[ks] = *lp<u32x4>(L, xa + ks * 64);
      __builtin_amdgcn_sched_barrier(0);
      stsc(__builtin_bit_cast(u32x4, x1) | tx, ws, x1a);
      __builtin_amdgcn_sched_barrier(0);
      const f32x4 d = mfma_chain<6>(wf, x);
      *lp<float>(L, pa) = c < 8 ? d[0] : d[2];
      *lp<float>(L, pa + 32) = c < 8 ? d[1] : d[3];
      CY(3);
    } else {
      int xr = L_X1 + (wave - 6) * D * 4 + lane * 16, sw = L_ST + (wave - 6) * 8;
      float K = *lp<float>(L, L_MB + (wave - 6) * 4);
      PIN(xr); PIN(sw); PIN(K);
      frags2();
      BAR();
      const f32x4 e0 = *lp<f32x4>(L, xr) - K, e1 = *lp<f32x4>(L, xr + 1024) - K;
      const float e2 = *lp<float>(L, xr + 2048 - lane * 12) - K;
      const f32x4 es = e0 + e1, eq = e0 * e0 + e1 * e1;
      const float s1 = hsum(es) + e2, s2 = hsum(eq) + e2 * e2;
      auto u = __builtin_amdgcn_permlane32_swap(fbits(s1), fbits(s2), false, false);
      const float ts = x16_sum(row_sum(bitsf(u[0]) + bitsf(u[1]))) * (1.f / D);
      auto w = __builtin_amdgcn_permlane32_swap(fbits(ts), fbits(ts), false, false);
      const float m1 = bitsf(w[0]), m2 = bitsf(w[1]);
      const f32x2 mv = rbf2(f32x2{K + m1, fmaxf(m2 - m1 * m1, 0.f)});
      *lp<f32x2>(L, sw) = f32x2{mv[0], rcp(rbf(__builtin_amdgcn_sqrtf(rbf(mv[1] + 1e-5f))))};
    }
    BAR();
    CY(4);
    const f32x4 t0 = *lp<f32x4>(L, ha), t1 = *lp<f32x4>(L, ha + 1152), t2 = *lp<f32x4>(L, ha + 2304);
    const f32x2 st = *lp<f32x2>(L, sta);
    __builtin_amdgcn_sched_barrier(0);
    const f32x4 t = (t0 + t1) + t2;
    const float hh = rbf(rbf(((t[0] + t[1]) - st[0] * t[2]) * st[1] + t[3]) + bias);
    const float rr = hh > 0.f ? hh : 0.f;
    CY(5);
    const u32 hb = pk2bf(rr * rr, rr * rr);
    const u32 w0 = __builtin_amdgcn_perm(hb, fbits(dpp<XOR2>(bitsf(hb))), psel);
    const u32x4 hf = {w0, fbits(dpp<0x12C>(bitsf(w0))), fbits(dpp<XOR8>(bitsf(w0))), fbits(dpp<ROR4>(bitsf(w0)))};
    f32x4 y[5];
#pragma unroll
    for (int i = 0; i < 5; ++i) y[i] = mfma(wf[6 + i], hf, f32x4{0, 0, 0, 0});
    const f32x4 y01 = (c & 2) ? y[1] : y[0], y23 = (c & 2) ? y[3] : y[2], y03 = (c & 4) ? y23 : y01;
    stsc(put_tag((c & 8) ? y[4] : y03, tb), ws, sa3);
    CY(6);
    STAMP(9);
  } else if (is_red) {
    const int o = tid & 31, bb = o >> 4, col = red * 16 + (o & 15);
    const rsrc_t ors = mkrs(p.out, 2 * D * 2);
    {
      BAR();
      STAMP(6);
      float gb = bflo(*lp<u16>(L, L_MODF + (bb * 2304 + 3 * D + col) * 2)), bias = *lp<float>(L, L_BIAS + (o & 15) * 4);
      int ra = L_RED + o * 32, oo = (bb * D + col) * 2, g3 = W_T3 + red * T3_RED + tid * 4;
      u32 tb = *lp<unsigned char>(L, L_TAG + (o & 7));
      PIN(gb); PIN(bias); PIN(ra); PIN(oo); PIN(tb); PIN(g3);
      float x1 = 0.f;
      u32x2 e2 = {0, 0};
      if (wave == 4) x1 = gather_sum_tag<1>(ws, W_T3 + red * T3_RED + 144 * 128 + o * 4, 0, tb);
      if (red == 0 && wave == 1) { ldsc(e2, ws, W_EPOCH, 0); PIN(e2); }
      if (T3DELAY >= 0) { BAR(); pause_until(clock32() + T3DELAY); }
      const float a = gather_sum_tag<9>(ws, g3, 2048, tb);
      STAMP(8);
      *lp<float>(L, ra + wave * 4) = x32_sum(a);
      BAR();
      if (wave == 4) {
        const f32x4 y = *lp<f32x4>(L, ra) + *lp<f32x4>(L, ra + 16);
        const float t = rbf(rbf(hsum(y)) + bias);
        __builtin_amdgcn_raw_buffer_store_b16((u16)f2bf(x1 + rbf(gb * t)), ors, oo, 0, 0);
      }
      if (red == 0 && tid == 64) {
        const u64 e = (((u64)e2[1] << 32) | e2[0]) + 1;
        stsc(u32x2{(u32)e, (u32)(e >> 32)}, ws, W_EPOCH);
      }
      STAMP(9);
    }
  }
}
}

extern "C" void kernel_launch(void* const* d_in, const int* in_sizes, int n_in, void* d_out, int out_size, void* d_ws, size_t ws_size, hipStream_t stream) {
  Params p;
  p.first[0] = d_in[11]; p.first[1] = d_in[4]; p.first[2] = d_in[3]; p.first[3] = d_in[6]; p.first[4] = p.first[5] = d_in[0];
  p.first[6] = d_in[10]; p.first[7] = d_in[0];
  p.kc = d_in[1]; p.vc = d_in[2]; p.b1 = d_in[7]; p.w2 = d_in[8]; p.b2 = d_in[9];
  p.out = d_out; p.ws = d_ws; p.w1 = d_in[6];
  for (int r = 0; r < 32; ++r)
    for (int w = 0; w < 8; ++w) {
      u32 kind = w, n4 = 0, lds = 0, so = 0, hold = 0;
      const bool single = r >= 12 && r < 21;
      if (w == 4 && !single) { kind = J_MOD; n4 = 6; lds = L_WM; }
      else if (w == 4 || (w < 3 && r >= 27)) {
        const int j = single ? 5 + (r - 12) / 3 : r - 27, t = single ? (r - 12) % 3 : w;
        kind = J_WQ; n4 = 6; lds = single ? L_WM : L_WT + w * 24576; so = t * 192 * HQ * DH * 2 + ((j + 3 * t) & 7) * 128;
      } else if (w < 4 && r < 9) { kind = J_WO; n4 = 4; lds = L_WT + w * 16384; so = w * 128 * D * 2 + r * 128; hold = r < 7; }
      else if (w < 4) { kind = J_W1; n4 = w < 3 && r < 27 ? 3 : 0; lds = L_WT + w * 12288; so = w * 192 * FF * 2; }
      p.job[r][w][0] = so; p.job[r][w][1] = kind * 8 | n4 << 8 | lds / 1024 << 12 | hold << 20;
    }
  fused_block<<<256, 512, 0, stream>>>(p);
}
```
